```python
import torch

def _softmax(x, axis=-1):
    return torch.softmax(x, dim=axis)
def _gelu(x):
    return torch.nn.functional.gelu(x, approximate="tanh")
def _silu(x):
    return torch.nn.functional.silu(x)
def _sigmoid(x):
    return torch.sigmoid(x)
def _layernorm(x, axis=-1, eps=1e-5):
    m = x.mean(dim=axis, keepdim=True)
    v = x.var(dim=axis, keepdim=True, unbiased=False)
    return (x - m) / torch.sqrt(v + eps)
def _scatter_add(tgt, idx, src, axis=0):
    return tgt.clone().index_add_(axis, idx, src)
def _segment_sum(src, seg, n):
    out = torch.zeros((n,) + src.shape[1:], dtype=src.dtype)
    return out.index_add_(0, seg, src)
def _pad(x, pw):
    flat = tuple(v for pair in reversed(pw) for v in pair)
    return torch.nn.functional.pad(x, flat)

kernel_name = 'gated_mix__sliding_block_cached__geglu_ffn__decode'

def setup_inputs():

    _w = (lambda a: (lambda t: t.to(torch.bfloat16) if t.is_floating_point() else t)(torch.as_tensor(a)))
    def _A_setup():
        B, Sq, D = 8, 1, 640
        Hq, Hkv, Sk, Dh = 32, 32, 4096, 192
        assert Hq % Hkv == 0, (Hq, Hkv)
        return {
            "x": _w((lambda *s: torch.randn(*s, dtype=torch.bfloat16) * 0.05)(B, Sq, D)),
            "k_cache": _w((lambda *s: torch.randn(*s, dtype=torch.bfloat16) * 6.4)(B, Hkv, Sk, Dh)),
            "v_cache": _w((lambda *s: torch.randn(*s, dtype=torch.bfloat16) * 0.2)(B, Hkv, Sk, Dh)),
            "wq": _w((lambda *s: torch.randn(*s, dtype=torch.bfloat16) * 6.4)(D, Hq * Dh) * (D ** -0.5)),
            "wo": _w((lambda *s: torch.randn(*s, dtype=torch.bfloat16) * 0.2)(Hq * Dh, D) * ((Hq * Dh) ** -0.5)),
        }
    _A_in = _A_setup()

    def _B_setup():
        B, S, D, F = 8, 1, 640, 1536
        return {
            "x": _w((lambda *s: torch.randn(*s, dtype=torch.bfloat16) * 0.05)(B, S, D)),
            "w_gate": _w((lambda *s: torch.randn(*s, dtype=torch.bfloat16) * 0.05)(D, F)),
            "w_up": _w((lambda *s: torch.randn(*s, dtype=torch.bfloat16) * 0.05)(D, F)),
            "w_down": _w((lambda *s: torch.randn(*s, dtype=torch.bfloat16) * 0.05)(F, D)),
        }
    _B_in = _B_setup()

    return {
        "A_x": _A_in["x"],
        "A_k_cache": _A_in["k_cache"],
        "A_v_cache": _A_in["v_cache"],
        "A_wq": _A_in["wq"],
        "A_wo": _A_in["wo"],
        "B_x": _B_in["x"],
        "B_w_gate": _B_in["w_gate"],
        "B_w_up": _B_in["w_up"],
        "B_w_down": _B_in["w_down"],
        "gate_w": _w((lambda *s: torch.randn(*s, dtype=torch.bfloat16) * 0.05)(640) * 16.0),
    }

def reference(A_x, A_k_cache, A_v_cache, A_wq, A_wo, B_x, B_w_gate, B_w_up, B_w_down, gate_w):
    def _A_ref(x, k_cache, v_cache, wq, wo):
        B, Sq, D = x.shape
        Hq, Dh = 32, 192
        Hkv, Sk = k_cache.shape[1], k_cache.shape[2]
        W = min(4096, max(256, Sk // 4))
        rep = Hq // Hkv
        q = (lambda x, *d: x.permute(*d))((x @ wq).reshape(B, Sq, Hq, Dh), 0, 2, 1, 3)
        k = (lambda x, n, axis: torch.repeat_interleave(x, n, dim=axis))(k_cache[:, :, Sk - W:, :], rep, axis=1)
        v = (lambda x, n, axis: torch.repeat_interleave(x, n, dim=axis))(v_cache[:, :, Sk - W:, :], rep, axis=1)
        scores = torch.einsum("bhqd,bhkd->bhqk", q, k) * (Dh ** -0.5)
        p = _softmax(scores, axis=-1)
        o = torch.einsum("bhqk,bhkd->bhqd", p, v)
        return (lambda x, *d: x.permute(*d))(o, 0, 2, 1, 3).reshape(B, Sq, Hq * Dh) @ wo

    def _B_ref(x, w_gate, w_up, w_down):
        return (_gelu(x @ w_gate) * (x @ w_up)) @ w_down

    x = A_x
    g = _sigmoid((x * gate_w).sum(-1, keepdims=True))
    a = _A_ref(x, A_k_cache, A_v_cache, A_wq, A_wo)
    b = _B_ref(x, B_w_gate, B_w_up, B_w_down)
    return g * a + (1 - g) * b
```

```cpp
#include <hip/hip_runtime.h>
#include <hip/hip_bf16.h>
#include <stdint.h>

namespace {
constexpr int NB = 8, DM = 640, NH = 32, DH = 192, SKV = 4096, WKV = 1024, FF = 1536;
constexpr int NT = 512, NWAVE = 8;
#define WIN 16
constexpr int NF = 20, NQ = 40, NK = 64, NV = 64;
constexpr int S_Q = NF, S_K = NF + NQ, S_V = S_K + NK, S_END = S_V + NV;

typedef unsigned u32x3 __attribute__((ext_vector_type(3)));
typedef unsigned u32x4 __attribute__((ext_vector_type(4)));
typedef __attribute__((ext_vector_type(2))) __bf16 bf16x2;

__device__ __forceinline__ float bfr(float x) { return __bfloat162float(__float2bfloat16(x)); }
__device__ __forceinline__ float blo(unsigned u) { return __builtin_bit_cast(float, u << 16); }
__device__ __forceinline__ float bhi(unsigned u) { return __builtin_bit_cast(float, u & 0xffff0000u); }
__device__ __forceinline__ float dot2(unsigned a, unsigned b, float c) {
  return __builtin_amdgcn_fdot2_f32_bf16(__builtin_bit_cast(bf16x2, a), __builtin_bit_cast(bf16x2, b), c, false);
}
template <int CTRL> __device__ __forceinline__ float dpp(float x) {
  return __builtin_bit_cast(float, __builtin_amdgcn_mov_dpp(__builtin_bit_cast(int, x), CTRL, 0xf, 0xf, true));
}
__device__ __forceinline__ float sum16(float x) {
  x += dpp<0xB1>(x);
  x += dpp<0x4E>(x);
  x += dpp<0x141>(x);
  x += dpp<0x140>(x);
  return x;
}
__device__ __forceinline__ float sum32(float x) {
  x = sum16(x);
  const float y = __builtin_bit_cast(float, __builtin_amdgcn_update_dpp(0, __builtin_bit_cast(int, x), 0x142, 0xa, 0xf, false));
  return x + y;
}
__device__ __forceinline__ float max16(float x) {
  x = fmaxf(x, dpp<0xB1>(x)); x = fmaxf(x, dpp<0x4E>(x)); x = fmaxf(x, dpp<0x141>(x)); x = fmaxf(x, dpp<0x140>(x));
  return x;
}
__device__ __forceinline__ float sum64(float x) {
  x = sum16(x);
  x += __shfl_xor(x, 16); x += __shfl_xor(x, 32);
  return x;
}
__device__ __forceinline__ float max64(float x) {
  x = max16(x);
  x = fmaxf(x, __shfl_xor(x, 16)); x = fmaxf(x, __shfl_xor(x, 32));
  return x;
}

#define PIN6(a) asm volatile("" : "+v"(a[0]), "+v"(a[1]), "+v"(a[2]), "+v"(a[3]), "+v"(a[4]), "+v"(a[5]))
struct Params {
  const __hip_bfloat16 *x, *k, *v, *wq, *wo, *wg, *wu, *wd, *gw;
  float* ws;
  __hip_bfloat16* out;
};

__global__ void __launch_bounds__(NT, 2) k1_attn(Params P) {
  __shared__ __attribute__((aligned(16))) float lds[640 + 1024 + 1024 + 16 * 192 + 192 + 64 + 8 * 96 + 12 * 640];
  float* xs = lds;
  float* sc = xs + 640;
  float* pb = sc + 1024;
  float* red = pb + 1024;
  float* of = red + 16 * 192;
  float* hf = of + 192;
  float* redF = hf + 64;
  float* red2 = redF + 8 * 96;

  const int bid = blockIdx.x;
  const int xcd = bid & 7, jj = bid >> 3;
  const int h = xcd * 4 + (jj >> 3), b = jj & 7;
  const int t = threadIdx.x, lane = t & 63, w = __builtin_amdgcn_readfirstlane(t >> 6);
  const int hf2 = lane >> 5, c = lane & 31;

  const size_t kvoff = ((size_t)(b * NH + h) * SKV + (SKV - WKV)) * DH;
  const auto rK = __builtin_amdgcn_make_buffer_rsrc((void*)(P.k + kvoff), 0, WKV * DH * 2, 0x00020000);
  const auto rV = __builtin_amdgcn_make_buffer_rsrc((void*)(P.v + kvoff), 0, WKV * DH * 2, 0x00020000);
  const auto rQ = __builtin_amdgcn_make_buffer_rsrc((void*)P.wq, 0, DM * NH * DH * 2, 0x00020000);
  const auto rG = __builtin_amdgcn_make_buffer_rsrc((void*)P.wg, 0, DM * FF * 2, 0x00020000);
  const auto rU = __builtin_amdgcn_make_buffer_rsrc((void*)P.wu, 0, DM * FF * 2, 0x00020000);

  const int voffF = (80 * w + (lane >> 3)) * (FF * 2) + h * 96 + (lane & 7) * 12;
  const int voffQ = (80 * w + hf2) * (NH * DH * 2) + h * 384 + c * 12;
  const int voffKV = (128 * w + hf2) * 384 + c * 12;

  auto LOAD = [&](int idx) -> u32x3 {
    if (idx < S_Q) {
      const int m = idx / 10, s = idx % 10;
      return __builtin_amdgcn_raw_buffer_load_b96(m ? rU : rG, voffF, s * 8 * FF * 2, 0);
    } else if (idx < S_K) {
      return __builtin_amdgcn_raw_buffer_load_b96(rQ, voffQ, (idx - S_Q) * 2 * NH * DH * 2, 0);
    } else if (idx < S_V) {
      return __builtin_amdgcn_raw_buffer_load_b96(rK, voffKV, (idx - S_K) * 768, 2);
    } else {
      return __builtin_amdgcn_raw_buffer_load_b96(rV, voffKV, (idx - S_V) * 768, 2);
    }
  };

  for (int i = t; i < DM; i += NT) xs[i] = __bfloat162float(P.x[b * DM + i]);
  u32x3 ring[WIN];
#pragma unroll
  for (int s = 0; s < WIN; ++s) ring[s] = LOAD(s);

  __syncthreads();

  float aF[2][6];
#pragma unroll
  for (int m = 0; m < 2; ++m)
#pragma unroll
    for (int k = 0; k < 6; ++k) aF[m][k] = 0.f;
#pragma unroll
  for (int s = 0; s < NF; ++s) {
    const u32x3 v = ring[s % WIN];
    const float xv = xs[80 * w + 8 * (s % 10) + (lane >> 3)];
    float* a = aF[s / 10];
    a[0] += xv * blo(v.x); a[1] += xv * bhi(v.x); a[2] += xv * blo(v.y); a[3] += xv * bhi(v.y); a[4] += xv * blo(v.z); a[5] += xv * bhi(v.z);
    PIN6(a);
    ring[s % WIN] = LOAD(s + WIN);
    __builtin_amdgcn_sched_barrier(0);
  }
  float aq[6] = {0.f, 0.f, 0.f, 0.f, 0.f, 0.f};
#pragma unroll
  for (int s = S_Q; s < S_K; ++s) {
    const u32x3 v = ring[s % WIN];
    const float xv = xs[80 * w + 2 * (s - S_Q) + hf2];
    aq[0] += xv * blo(v.x); aq[1] += xv * bhi(v.x); aq[2] += xv * blo(v.y); aq[3] += xv * bhi(v.y); aq[4] += xv * blo(v.z); aq[5] += xv * bhi(v.z);
    PIN6(aq);
    ring[s % WIN] = LOAD(s + WIN);
    __builtin_amdgcn_sched_barrier(0);
  }
#pragma unroll
  for (int m = 0; m < 2; ++m)
#pragma unroll
    for (int k = 0; k < 6; ++k) {
      float vv = aF[m][k];
      vv += __shfl_xor(vv, 8); vv += __shfl_xor(vv, 16); vv += __shfl_xor(vv, 32);
      if (lane < 8) redF[(w * 2 + m) * 48 + lane * 6 + k] = vv;
    }
#pragma unroll
  for (int k = 0; k < 6; ++k) red[(w * 2 + hf2) * 192 + 6 * c + k] = aq[k];
  __syncthreads();
  if (t < 192) {
    float s = 0.f;
#pragma unroll
    for (int i = 0; i < 16; ++i) s += red[i * 192 + t];
    of[t] = bfr(s);
  } else if (t < 240) {
    const int cc = t - 192;
    float g = 0.f, u = 0.f;
#pragma unroll
    for (int i = 0; i < 8; ++i) { g += redF[(i * 2) * 48 + cc]; u += redF[(i * 2 + 1) * 48 + cc]; }
    g = bfr(g); u = bfr(u);
    const float inner = 0.7978845608028654f * (g + 0.044715f * g * g * g);
    const float ge = bfr(0.5f * g * (1.f + tanhf(inner)));
    hf[cc] = bfr(ge * u);
  }
  __syncthreads();
  unsigned q0, q1, q2;
  {
    const float* qq = of + 6 * c;
    auto pk = [](float lo, float hi) { return (__builtin_bit_cast(unsigned, lo) >> 16) | (__builtin_bit_cast(unsigned, hi) & 0xffff0000u); };
    q0 = pk(qq[0], qq[1]); q1 = pk(qq[2], qq[3]); q2 = pk(qq[4], qq[5]);
  }

  constexpr float SCALE = 0.07216878364870322f;
  constexpr int NIT = NK / WIN;
#pragma unroll 1
  for (int it = 0; it < NIT; ++it) {
    const bool last = (it == NIT - 1);
#pragma unroll
    for (int j = 0; j < WIN; ++j) {
      const int slot = (S_K + j) % WIN;
      const u32x3 v = ring[slot];
      float d = dot2(v.x, q0, 0.f); d = dot2(v.y, q1, d); d = dot2(v.z, q2, d);
      d = sum32(d);
      d = bfr(bfr(d) * SCALE);
      if (c == 16) sc[128 * w + 2 * (it * WIN + j) + hf2] = d;
      ring[slot] = __builtin_amdgcn_raw_buffer_load_b96(last ? rV : rK, voffKV, last ? j * 768 : ((it + 1) * WIN + j) * 768, 2);
      __builtin_amdgcn_sched_barrier(0);
    }
  }
  __syncthreads();
  {
    float sv[16];
#pragma unroll
    for (int i = 0; i < 4; ++i) {
      const float4 f = *reinterpret_cast<const float4*>(sc + lane * 16 + i * 4);
      sv[4 * i] = f.x; sv[4 * i + 1] = f.y; sv[4 * i + 2] = f.z; sv[4 * i + 3] = f.w;
    }
    float m = sv[0];
#pragma unroll
    for (int i = 1; i < 16; ++i) m = fmaxf(m, sv[i]);
    m = max64(m);
    float l = 0.f;
#pragma unroll
    for (int i = 0; i < 16; ++i) l += __expf(sv[i] - m);
    l = sum64(l);
    const float inv = 1.f / l;
    pb[128 * w + lane] = bfr(__expf(sc[128 * w + lane] - m) * inv);
    pb[128 * w + 64 + lane] = bfr(__expf(sc[128 * w + 64 + lane] - m) * inv);
  }
  float acc[6] = {0.f, 0.f, 0.f, 0.f, 0.f, 0.f};
#pragma unroll 1
  for (int it = 0; it < NIT - 1; ++it) {
#pragma unroll
    for (int j = 0; j < WIN; ++j) {
      const int slot = (S_V + j) % WIN;
      const u32x3 v = ring[slot];
      const float p = pb[128 * w + 2 * (it * WIN + j) + hf2];
      acc[0] += p * blo(v.x); acc[1] += p * bhi(v.x); acc[2] += p * blo(v.y); acc[3] += p * bhi(v.y); acc[4] += p * blo(v.z); acc[5] += p * bhi(v.z);
      PIN6(acc);
      ring[slot] = __builtin_amdgcn_raw_buffer_load_b96(rV, voffKV, ((it + 1) * WIN + j) * 768, 2);
      __builtin_amdgcn_sched_barrier(0);
    }
  }
#pragma unroll
  for (int j = 0; j < WIN; ++j) {
    const int slot = (S_V + j) % WIN;
    const u32x3 v = ring[slot];
    const float p = pb[128 * w + 2 * ((NIT - 1) * WIN + j) + hf2];
    acc[0] += p * blo(v.x); acc[1] += p * bhi(v.x); acc[2] += p * blo(v.y); acc[3] += p * bhi(v.y); acc[4] += p * blo(v.z); acc[5] += p * bhi(v.z);
  }
#pragma unroll
  for (int k = 0; k < 6; ++k) red[(w * 2 + hf2) * 192 + 6 * c + k] = acc[k];
  __syncthreads();
  if (t < 192) {
    float s = 0.f;
#pragma unroll
    for (int i = 0; i < 16; ++i) s += red[i * 192 + t];
    of[t] = bfr(s);
  }
  __syncthreads();
  if (t < 480) {
    const int cc = t % 80, rg = t / 80;
    const auto rO = __builtin_amdgcn_make_buffer_rsrc((void*)P.wo, 0, NH * DH * DM * 2, 0x00020000);
    const auto rD = __builtin_amdgcn_make_buffer_rsrc((void*)P.wd, 0, FF * DM * 2, 0x00020000);
    float a8[8], b8[8];
#pragma unroll
    for (int k = 0; k < 8; ++k) { a8[k] = 0.f; b8[k] = 0.f; }
    const int vo = ((DH * h + rg) * 80 + cc) * 16;
#pragma unroll
    for (int j = 0; j < 32; ++j) {
      const u32x4 v = __builtin_amdgcn_raw_buffer_load_b128(rO, vo, j * 6 * 1280, 0);
      const float ov = of[rg + 6 * j];
      a8[0] += ov * blo(v.x); a8[1] += ov * bhi(v.x); a8[2] += ov * blo(v.y); a8[3] += ov * bhi(v.y);
      a8[4] += ov * blo(v.z); a8[5] += ov * bhi(v.z); a8[6] += ov * blo(v.w); a8[7] += ov * bhi(v.w);
    }
    const int vd = ((48 * h + rg) * 80 + cc) * 16;
#pragma unroll
    for (int j = 0; j < 8; ++j) {
      const u32x4 v = __builtin_amdgcn_raw_buffer_load_b128(rD, vd, j * 6 * 1280, 0);
      const float hv = hf[rg + 6 * j];
      b8[0] += hv * blo(v.x); b8[1] += hv * bhi(v.x); b8[2] += hv * blo(v.y); b8[3] += hv * bhi(v.y);
      b8[4] += hv * blo(v.z); b8[5] += hv * bhi(v.z); b8[6] += hv * blo(v.w); b8[7] += hv * bhi(v.w);
    }
    float4* d0 = reinterpret_cast<float4*>(red2 + rg * 640 + cc * 8);
    d0[0] = make_float4(a8[0], a8[1], a8[2], a8[3]); d0[1] = make_float4(a8[4], a8[5], a8[6], a8[7]);
    float4* d1 = reinterpret_cast<float4*>(red2 + (6 + rg) * 640 + cc * 8);
    d1[0] = make_float4(b8[0], b8[1], b8[2], b8[3]); d1[1] = make_float4(b8[4], b8[5], b8[6], b8[7]);
  }
  __syncthreads();
  for (int i = t; i < 1280; i += NT) {
    const int m = i / 640, cc = i % 640;
    float s = 0.f;
#pragma unroll
    for (int r = 0; r < 6; ++r) s += red2[(m * 6 + r) * 640 + cc];
    P.ws[((size_t)m * NB * NH + b * NH + h) * 640 + cc] = s;
  }
}

__global__ void __launch_bounds__(128) k2_mix(Params P) {
  __shared__ float gs[2];
  const int b = blockIdx.x / 5, cc = (blockIdx.x % 5) * 128 + threadIdx.x;
  float s = 0.f;
  for (int i = threadIdx.x; i < DM; i += 128) s += bfr(__bfloat162float(P.x[b * DM + i]) * __bfloat162float(P.gw[i]));
  s = sum64(s);
  if ((threadIdx.x & 63) == 0) gs[threadIdx.x >> 6] = s;
  __syncthreads();
  const float z = bfr(gs[0] + gs[1]);
  const float g = bfr(1.f / (1.f + __expf(-z)));
  float a = 0.f, bb = 0.f;
#pragma unroll 8
  for (int hh = 0; hh < NH; ++hh) {
    a += P.ws[((size_t)b * NH + hh) * 640 + cc];
    bb += P.ws[((size_t)NB * NH + b * NH + hh) * 640 + cc];
  }
  a = bfr(a); bb = bfr(bb);
  const float r = bfr(g * a) + bfr(bfr(1.f - g) * bb);
  P.out[b * DM + cc] = __float2bfloat16(r);
}
}

extern "C" void kernel_launch(void* const* d_in, const int* in_sizes, int n_in, void* d_out, int out_size, void* d_ws, size_t ws_size,
                              hipStream_t stream) {
  Params P;
  P.x = (const __hip_bfloat16*)d_in[0];
  P.k = (const __hip_bfloat16*)d_in[1];
  P.v = (const __hip_bfloat16*)d_in[2];
  P.wq = (const __hip_bfloat16*)d_in[3];
  P.wo = (const __hip_bfloat16*)d_in[4];
  P.wg = (const __hip_bfloat16*)d_in[6];
  P.wu = (const __hip_bfloat16*)d_in[7];
  P.wd = (const __hip_bfloat16*)d_in[8];
  P.gw = (const __hip_bfloat16*)d_in[9];
  P.ws = (float*)d_ws;
  P.out = (__hip_bfloat16*)d_out;
  k1_attn<<<256, NT, 0, stream>>>(P);
  k2_mix<<<40, 128, 0, stream>>>(P);
}
```

```cpp
#include <hip/hip_runtime.h>
#include <hip/hip_bf16.h>
#include <stdint.h>

namespace {
constexpr int NB = 8, DM = 640, NH = 32, DH = 192, SKV = 4096, WKV = 1024, FF = 1536;
constexpr int NT = 512, NWAVE = 8;
#define WIN 16
constexpr int NF = 20, NQ = 40, NK = 64, NV = 64;
constexpr int S_Q = 0, S_K = 0, S_V = S_K + NK, S_END = S_V + NV;

typedef unsigned u32x3 __attribute__((ext_vector_type(3)));
typedef unsigned u32x4 __attribute__((ext_vector_type(4)));
typedef __attribute__((ext_vector_type(2))) __bf16 bf16x2;

__device__ __forceinline__ float bfr(float x) { return __bfloat162float(__float2bfloat16(x)); }
__device__ __forceinline__ float blo(unsigned u) { return __builtin_bit_cast(float, u << 16); }
__device__ __forceinline__ float bhi(unsigned u) { return __builtin_bit_cast(float, u & 0xffff0000u); }
__device__ __forceinline__ float dot2(unsigned a, unsigned b, float c) {
  return __builtin_amdgcn_fdot2_f32_bf16(__builtin_bit_cast(bf16x2, a), __builtin_bit_cast(bf16x2, b), c, false);
}
template <int CTRL> __device__ __forceinline__ float dpp(float x) {
  return __builtin_bit_cast(float, __builtin_amdgcn_mov_dpp(__builtin_bit_cast(int, x), CTRL, 0xf, 0xf, true));
}
__device__ __forceinline__ float sum16(float x) {
  x += dpp<0xB1>(x);
  x += dpp<0x4E>(x);
  x += dpp<0x141>(x);
  x += dpp<0x140>(x);
  return x;
}
__device__ __forceinline__ float sum32(float x) {
  x = sum16(x);
  const float y = __builtin_bit_cast(float, __builtin_amdgcn_update_dpp(0, __builtin_bit_cast(int, x), 0x142, 0xa, 0xf, false));
  return x + y;
}
__device__ __forceinline__ float max16(float x) {
  x = fmaxf(x, dpp<0xB1>(x)); x = fmaxf(x, dpp<0x4E>(x)); x = fmaxf(x, dpp<0x141>(x)); x = fmaxf(x, dpp<0x140>(x));
  return x;
}
__device__ __forceinline__ float sum64(float x) {
  x = sum16(x);
  x += __shfl_xor(x, 16); x += __shfl_xor(x, 32);
  return x;
}
__device__ __forceinline__ float max64(float x) {
  x = max16(x);
  x = fmaxf(x, __shfl_xor(x, 16)); x = fmaxf(x, __shfl_xor(x, 32));
  return x;
}

#define PIN6(a) asm volatile("" : "+v"(a[0]), "+v"(a[1]), "+v"(a[2]), "+v"(a[3]), "+v"(a[4]), "+v"(a[5]))
#define ST(i)
struct Params {
  const __hip_bfloat16 *x, *k, *v, *wq, *wo, *wg, *wu, *wd, *gw;
  float* ws;
  __hip_bfloat16* out;
};

constexpr size_t WS_PART = 2 * NB * NH * 640;

constexpr int NCOL = NH * DH + 2 * FF;
__global__ void __launch_bounds__(512) k0_proj(Params P) {
  __shared__ __attribute__((aligned(16))) float xs0[80 * 8];
  __shared__ __attribute__((aligned(16))) float redk[8 * 8 * 512];
  const int tile = blockIdx.x >> 3, rg = blockIdx.x & 7;
  const int t = threadIdx.x, lane = t & 63, w = __builtin_amdgcn_readfirstlane(t >> 6);
  const __hip_bfloat16* W; int ld, col0;
  if (tile < 12) { W = P.wq; ld = NH * DH; col0 = tile * 512; }
  else if (tile < 15) { W = P.wg; ld = FF; col0 = (tile - 12) * 512; }
  else { W = P.wu; ld = FF; col0 = (tile - 15) * 512; }
  const auto rW = __builtin_amdgcn_make_buffer_rsrc((void*)W, 0, DM * ld * 2, 0x00020000);
  const int voff = ((rg * 80 + w * 10) * ld + col0 + lane * 8) * 2;
  u32x4 vv[10];
#pragma unroll
  for (int i = 0; i < 10; ++i) vv[i] = __builtin_amdgcn_raw_buffer_load_b128(rW, voff, i * ld * 2, 0);
  for (int i = t; i < 640; i += 512) { const int r = i >> 3, bb = i & 7; xs0[i] = __bfloat162float(P.x[bb * DM + rg * 80 + r]); }
  __syncthreads();
  float acc[8][8];
#pragma unroll
  for (int bb = 0; bb < 8; ++bb)
#pragma unroll
    for (int k = 0; k < 8; ++k) acc[bb][k] = 0.f;
#pragma unroll
  for (int i = 0; i < 10; ++i) {
    const float4 xa = *reinterpret_cast<const float4*>(xs0 + (w * 10 + i) * 8), xb = *reinterpret_cast<const float4*>(xs0 + (w * 10 + i) * 8 + 4);
    const float xv[8] = {xa.x, xa.y, xa.z, xa.w, xb.x, xb.y, xb.z, xb.w};
    const float f[8] = {blo(vv[i].x), bhi(vv[i].x), blo(vv[i].y), bhi(vv[i].y), blo(vv[i].z), bhi(vv[i].z), blo(vv[i].w), bhi(vv[i].w)};
#pragma unroll
    for (int bb = 0; bb < 8; ++bb)
#pragma unroll
      for (int k = 0; k < 8; ++k) acc[bb][k] += xv[bb] * f[k];
  }
#pragma unroll
  for (int bb = 0; bb < 8; ++bb) {
    float4* d = reinterpret_cast<float4*>(redk + (w * 8 + bb) * 512 + lane * 8);
    d[0] = make_float4(acc[bb][0], acc[bb][1], acc[bb][2], acc[bb][3]); d[1] = make_float4(acc[bb][4], acc[bb][5], acc[bb][6], acc[bb][7]);
  }
  __syncthreads();
  float* part = P.ws + WS_PART;
#pragma unroll
  for (int i = 0; i < 8; ++i) {
    const int idx = i * 512 + t, bb = idx >> 9, cc = idx & 511;
    float s = 0.f;
#pragma unroll
    for (int ww = 0; ww < 8; ++ww) s += redk[(ww * 8 + bb) * 512 + cc];
    part[((size_t)rg * 8 + bb) * NCOL + tile * 512 + cc] = s;
  }
}

__global__ void __launch_bounds__(NT, 2) k1_attn(Params P) {
  __shared__ __attribute__((aligned(16))) float lds[640 + 1024 + 1024 + 16 * 192 + 192 + 64 + 8 * 96 + 12 * 640];
  float* xs = lds;
  float* sc = xs + 640;
  float* pb = sc + 1024;
  float* red = pb + 1024;
  float* of = red + 16 * 192;
  float* hf = of + 192;
  float* redF = hf + 64;
  float* red2 = redF + 8 * 96;

  ST(0);
  const int bid = blockIdx.x;
  const int xcd = bid & 7, jj = bid >> 3;
  const int h = xcd * 4 + (jj >> 3), b = jj & 7;
  const int t = threadIdx.x, lane = t & 63, w = __builtin_amdgcn_readfirstlane(t >> 6);
  const int hf2 = lane >> 5, c = lane & 31;

  const size_t kvoff = ((size_t)(b * NH + h) * SKV + (SKV - WKV)) * DH;
  const auto rK = __builtin_amdgcn_make_buffer_rsrc((void*)(P.k + kvoff), 0, WKV * DH * 2, 0x00020000);
  const auto rV = __builtin_amdgcn_make_buffer_rsrc((void*)(P.v + kvoff), 0, WKV * DH * 2, 0x00020000);
  const auto rQ = __builtin_amdgcn_make_buffer_rsrc((void*)P.wq, 0, DM * NH * DH * 2, 0x00020000);
  const auto rG = __builtin_amdgcn_make_buffer_rsrc((void*)P.wg, 0, DM * FF * 2, 0x00020000);
  const auto rU = __builtin_amdgcn_make_buffer_rsrc((void*)P.wu, 0, DM * FF * 2, 0x00020000);

  const int voffF = (80 * w + (lane >> 3)) * (FF * 2) + h * 96 + (lane & 7) * 12;
  const int voffQ = (80 * w + hf2) * (NH * DH * 2) + h * 384 + c * 12;
  const int voffKV = (128 * w + hf2) * 384 + c * 12;

  auto LOAD = [&](int idx) -> u32x3 {
    if (idx < S_V) {
      return __builtin_amdgcn_raw_buffer_load_b96(rK, voffKV, (idx - S_K) * 768, 2);
    } else {
      return __builtin_amdgcn_raw_buffer_load_b96(rV, voffKV, (idx - S_V) * 768, 2);
    }
  };

  u32x3 ring[WIN];
#pragma unroll
  for (int s = 0; s < WIN; ++s) ring[s] = LOAD(s);

  {
    const float* part = P.ws + WS_PART;
    if (t < 192) {
      float sum = 0.f;
#pragma unroll
      for (int r = 0; r < 8; ++r) sum += part[((size_t)r * 8 + b) * NCOL + h * DH + t];
      of[t] = bfr(sum);
    } else if (t < 240) {
      const int cc = t - 192;
      float g = 0.f, u = 0.f;
#pragma unroll
      for (int r = 0; r < 8; ++r) { g += part[((size_t)r * 8 + b) * NCOL + NH * DH + h * 48 + cc]; u += part[((size_t)r * 8 + b) * NCOL + NH * DH + FF + h * 48 + cc]; }
      g = bfr(g); u = bfr(u);
      const float inner = 0.7978845608028654f * (g + 0.044715f * g * g * g);
      const float ge = bfr(0.5f * g * (1.f + tanhf(inner)));
      hf[cc] = bfr(ge * u);
    }
  }
  __syncthreads();
  unsigned q0, q1, q2;
  {
    const float* qq = of + 6 * c;
    auto pk = [](float lo, float hi) { return (__builtin_bit_cast(unsigned, lo) >> 16) | (__builtin_bit_cast(unsigned, hi) & 0xffff0000u); };
    q0 = pk(qq[0], qq[1]); q1 = pk(qq[2], qq[3]); q2 = pk(qq[4], qq[5]);
  }

  ST(1);
  constexpr float SCALE = 0.07216878364870322f;
  constexpr int NIT = NK / WIN;
#pragma unroll 1
  for (int it = 0; it < NIT; ++it) {
    const bool last = (it == NIT - 1);
#pragma unroll
    for (int j = 0; j < WIN; ++j) {
      const int slot = (S_K + j) % WIN;
      const u32x3 v = ring[slot];
      float d = dot2(v.x, q0, 0.f); d = dot2(v.y, q1, d); d = dot2(v.z, q2, d);
      d = sum32(d);
      d = bfr(bfr(d) * SCALE);
      if (c == 16) sc[128 * w + 2 * (it * WIN + j) + hf2] = d;
      ring[slot] = __builtin_amdgcn_raw_buffer_load_b96(last ? rV : rK, voffKV, last ? j * 768 : ((it + 1) * WIN + j) * 768, 2);
      __builtin_amdgcn_sched_barrier(0);
    }
  }
  __syncthreads();
  ST(2);
  {
    float sv[16];
#pragma unroll
    for (int i = 0; i < 4; ++i) {
      const float4 f = *reinterpret_cast<const float4*>(sc + lane * 16 + i * 4);
      sv[4 * i] = f.x; sv[4 * i + 1] = f.y; sv[4 * i + 2] = f.z; sv[4 * i + 3] = f.w;
    }
    float m = sv[0];
#pragma unroll
    for (int i = 1; i < 16; ++i) m = fmaxf(m, sv[i]);
    m = max64(m);
    float l = 0.f;
#pragma unroll
    for (int i = 0; i < 16; ++i) l += __expf(sv[i] - m);
    l = sum64(l);
    const float inv = 1.f / l;
    pb[128 * w + lane] = bfr(__expf(sc[128 * w + lane] - m) * inv);
    pb[128 * w + 64 + lane] = bfr(__expf(sc[128 * w + 64 + lane] - m) * inv);
  }
  float acc[6] = {0.f, 0.f, 0.f, 0.f, 0.f, 0.f};
#pragma unroll 1
  for (int it = 0; it < NIT - 1; ++it) {
#pragma unroll
    for (int j = 0; j < WIN; ++j) {
      const int slot = (S_V + j) % WIN;
      const u32x3 v = ring[slot];
      const float p = pb[128 * w + 2 * (it * WIN + j) + hf2];
      acc[0] += p * blo(v.x); acc[1] += p * bhi(v.x); acc[2] += p * blo(v.y); acc[3] += p * bhi(v.y); acc[4] += p * blo(v.z); acc[5] += p * bhi(v.z);
      PIN6(acc);
      ring[slot] = __builtin_amdgcn_raw_buffer_load_b96(rV, voffKV, ((it + 1) * WIN + j) * 768, 2);
      __builtin_amdgcn_sched_barrier(0);
    }
  }
#pragma unroll
  for (int j = 0; j < WIN; ++j) {
    const int slot = (S_V + j) % WIN;
    const u32x3 v = ring[slot];
    const float p = pb[128 * w + 2 * ((NIT - 1) * WIN + j) + hf2];
    acc[0] += p * blo(v.x); acc[1] += p * bhi(v.x); acc[2] += p * blo(v.y); acc[3] += p * bhi(v.y); acc[4] += p * blo(v.z); acc[5] += p * bhi(v.z);
  }
#pragma unroll
  for (int k = 0; k < 6; ++k) red[(w * 2 + hf2) * 192 + 6 * c + k] = acc[k];
  __syncthreads();
  if (t < 192) {
    float s = 0.f;
#pragma unroll
    for (int i = 0; i < 16; ++i) s += red[i * 192 + t];
    of[t] = bfr(s);
  }
  __syncthreads();
  ST(3);
  if (t < 480) {
    const int cc = t % 80, rg = t / 80;
    const auto rO = __builtin_amdgcn_make_buffer_rsrc((void*)P.wo, 0, NH * DH * DM * 2, 0x00020000);
    const auto rD = __builtin_amdgcn_make_buffer_rsrc((void*)P.wd, 0, FF * DM * 2, 0x00020000);
    float a8[8], b8[8];
#pragma unroll
    for (int k = 0; k < 8; ++k) { a8[k] = 0.f; b8[k] = 0.f; }
    const int vo = ((DH * h + rg) * 80 + cc) * 16;
#pragma unroll
    for (int j = 0; j < 32; ++j) {
      const u32x4 v = __builtin_amdgcn_raw_buffer_load_b128(rO, vo, j * 6 * 1280, 0);
      const float ov = of[rg + 6 * j];
      a8[0] += ov * blo(v.x); a8[1] += ov * bhi(v.x); a8[2] += ov * blo(v.y); a8[3] += ov * bhi(v.y);
      a8[4] += ov * blo(v.z); a8[5] += ov * bhi(v.z); a8[6] += ov * blo(v.w); a8[7] += ov * bhi(v.w);
    }
    const int vd = ((48 * h + rg) * 80 + cc) * 16;
#pragma unroll
    for (int j = 0; j < 8; ++j) {
      const u32x4 v = __builtin_amdgcn_raw_buffer_load_b128(rD, vd, j * 6 * 1280, 0);
      const float hv = hf[rg + 6 * j];
      b8[0] += hv * blo(v.x); b8[1] += hv * bhi(v.x); b8[2] += hv * blo(v.y); b8[3] += hv * bhi(v.y);
      b8[4] += hv * blo(v.z); b8[5] += hv * bhi(v.z); b8[6] += hv * blo(v.w); b8[7] += hv * bhi(v.w);
    }
    float4* d0 = reinterpret_cast<float4*>(red2 + rg * 640 + cc * 8);
    d0[0] = make_float4(a8[0], a8[1], a8[2], a8[3]); d0[1] = make_float4(a8[4], a8[5], a8[6], a8[7]);
    float4* d1 = reinterpret_cast<float4*>(red2 + (6 + rg) * 640 + cc * 8);
    d1[0] = make_float4(b8[0], b8[1], b8[2], b8[3]); d1[1] = make_float4(b8[4], b8[5], b8[6], b8[7]);
  }
  __syncthreads();
  for (int i = t; i < 1280; i += NT) {
    const int m = i / 640, cc = i % 640;
    float s = 0.f;
#pragma unroll
    for (int r = 0; r < 6; ++r) s += red2[(m * 6 + r) * 640 + cc];
    P.ws[((size_t)m * NB * NH + b * NH + h) * 640 + cc] = s;
  }
  ST(4);
}

__global__ void __launch_bounds__(128) k2_mix(Params P) {
  __shared__ float gs[2];
  const int b = blockIdx.x / 5, cc = (blockIdx.x % 5) * 128 + threadIdx.x;
  float s = 0.f;
  for (int i = threadIdx.x; i < DM; i += 128) s += bfr(__bfloat162float(P.x[b * DM + i]) * __bfloat162float(P.gw[i]));
  s = sum64(s);
  if ((threadIdx.x & 63) == 0) gs[threadIdx.x >> 6] = s;
  __syncthreads();
  const float z = bfr(gs[0] + gs[1]);
  const float g = bfr(1.f / (1.f + __expf(-z)));
  float a = 0.f, bb = 0.f;
#pragma unroll 8
  for (int hh = 0; hh < NH; ++hh) {
    a += P.ws[((size_t)b * NH + hh) * 640 + cc];
    bb += P.ws[((size_t)NB * NH + b * NH + hh) * 640 + cc];
  }
  a = bfr(a); bb = bfr(bb);
  const float r = bfr(g * a) + bfr(bfr(1.f - g) * bb);
  P.out[b * DM + cc] = __float2bfloat16(r);
}
}

extern "C" void kernel_launch(void* const* d_in, const int* in_sizes, int n_in, void* d_out, int out_size, void* d_ws, size_t ws_size,
                              hipStream_t stream) {
  Params P;
  P.x = (const __hip_bfloat16*)d_in[0];
  P.k = (const __hip_bfloat16*)d_in[1];
  P.v = (const __hip_bfloat16*)d_in[2];
  P.wq = (const __hip_bfloat16*)d_in[3];
  P.wo = (const __hip_bfloat16*)d_in[4];
  P.wg = (const __hip_bfloat16*)d_in[6];
  P.wu = (const __hip_bfloat16*)d_in[7];
  P.wd = (const __hip_bfloat16*)d_in[8];
  P.gw = (const __hip_bfloat16*)d_in[9];
  P.ws = (float*)d_ws;
  P.out = (__hip_bfloat16*)d_out;
  k0_proj<<<144, 512, 0, stream>>>(P);
  k1_attn<<<256, NT, 0, stream>>>(P);
  k2_mix<<<40, 128, 0, stream>>>(P);
}
```

```cpp
#include <hip/hip_runtime.h>
#include <hip/hip_bf16.h>
#include <stdint.h>

namespace {
constexpr int NB = 8, DM = 640, NH = 32, DH = 192, SKV = 4096, WKV = 1024, FF = 1536;
constexpr int NT = 512, NWAVE = 8;
#define WIN 16
constexpr int NF = 20, NQ = 40, NK = 64, NV = 64;
constexpr int S_Q = NF, S_K = NF + NQ, S_V = S_K + NK, S_END = S_V + NV;

typedef unsigned u32x3 __attribute__((ext_vector_type(3)));
typedef unsigned u32x4 __attribute__((ext_vector_type(4)));
typedef __attribute__((ext_vector_type(2))) __bf16 bf16x2;

__device__ __forceinline__ float bfr(float x) { return __bfloat162float(__float2bfloat16(x)); }
__device__ __forceinline__ float blo(unsigned u) { return __builtin_bit_cast(float, u << 16); }
__device__ __forceinline__ float bhi(unsigned u) { return __builtin_bit_cast(float, u & 0xffff0000u); }
__device__ __forceinline__ float dot2(unsigned a, unsigned b, float c) {
  return __builtin_amdgcn_fdot2_f32_bf16(__builtin_bit_cast(bf16x2, a), __builtin_bit_cast(bf16x2, b), c, false);
}
template <int CTRL> __device__ __forceinline__ float dpp(float x) {
  return __builtin_bit_cast(float, __builtin_amdgcn_mov_dpp(__builtin_bit_cast(int, x), CTRL, 0xf, 0xf, true));
}
__device__ __forceinline__ float sum16(float x) {
  x += dpp<0xB1>(x);
  x += dpp<0x4E>(x);
  x += dpp<0x141>(x);
  x += dpp<0x140>(x);
  return x;
}
__device__ __forceinline__ float sum32(float x) {
  x = sum16(x);
  const float y = __builtin_bit_cast(float, __builtin_amdgcn_update_dpp(0, __builtin_bit_cast(int, x), 0x142, 0xa, 0xf, false));
  return x + y;
}
__device__ __forceinline__ float max16(float x) {
  x = fmaxf(x, dpp<0xB1>(x)); x = fmaxf(x, dpp<0x4E>(x)); x = fmaxf(x, dpp<0x141>(x)); x = fmaxf(x, dpp<0x140>(x));
  return x;
}
__device__ __forceinline__ float sum64(float x) {
  x = sum16(x);
  x += __shfl_xor(x, 16); x += __shfl_xor(x, 32);
  return x;
}
__device__ __forceinline__ float max64(float x) {
  x = max16(x);
  x = fmaxf(x, __shfl_xor(x, 16)); x = fmaxf(x, __shfl_xor(x, 32));
  return x;
}

#define PIN6(a) asm volatile("" : "+v"(a[0]), "+v"(a[1]), "+v"(a[2]), "+v"(a[3]), "+v"(a[4]), "+v"(a[5]))
#define ST(i)
struct Params {
  const __hip_bfloat16 *x, *k, *v, *wq, *wo, *wg, *wu, *wd, *gw;
  float* ws;
  __hip_bfloat16* out;
};

__global__ void __launch_bounds__(NT, 2) k1_attn(Params P) {
  __shared__ __attribute__((aligned(16))) float lds[640 + 1024 + 1024 + 16 * 192 + 192 + 64 + 8 * 96 + 12 * 640];
  float* xs = lds;
  float* sc = xs + 640;
  float* pb = sc + 1024;
  float* red = pb + 1024;
  float* of = red + 16 * 192;
  float* hf = of + 192;
  float* redF = hf + 64;
  float* red2 = redF + 8 * 96;

  ST(0);
  const int bid = blockIdx.x;
  const int xcd = bid & 7, jj = bid >> 3;
  const int h = xcd * 4 + (jj >> 3), b = jj & 7;
  const int t = threadIdx.x, lane = t & 63, w = __builtin_amdgcn_readfirstlane(t >> 6);
  const int hf2 = lane >> 5, c = lane & 31;

  const size_t kvoff = ((size_t)(b * NH + h) * SKV + (SKV - WKV)) * DH;
  const auto rK = __builtin_amdgcn_make_buffer_rsrc((void*)(P.k + kvoff), 0, WKV * DH * 2, 0x00020000);
  const auto rV = __builtin_amdgcn_make_buffer_rsrc((void*)(P.v + kvoff), 0, WKV * DH * 2, 0x00020000);
  const auto rQ = __builtin_amdgcn_make_buffer_rsrc((void*)P.wq, 0, DM * NH * DH * 2, 0x00020000);
  const auto rG = __builtin_amdgcn_make_buffer_rsrc((void*)P.wg, 0, DM * FF * 2, 0x00020000);
  const auto rU = __builtin_amdgcn_make_buffer_rsrc((void*)P.wu, 0, DM * FF * 2, 0x00020000);

  const int voffF = (80 * w + (lane >> 3)) * (FF * 2) + h * 96 + (lane & 7) * 12;
  const int voffQ = (80 * w + hf2) * (NH * DH * 2) + h * 384 + c * 12;
  const int voffKV = (128 * w + hf2) * 384 + c * 12;

  auto LOAD = [&](int idx) -> u32x3 {
    if (idx < S_Q) {
      const int m = idx / 10, s = idx % 10;
      return __builtin_amdgcn_raw_buffer_load_b96(m ? rU : rG, voffF, s * 8 * FF * 2, 0);
    } else if (idx < S_K) {
      return __builtin_amdgcn_raw_buffer_load_b96(rQ, voffQ, (idx - S_Q) * 2 * NH * DH * 2, 0);
    } else if (idx < S_V) {
      return __builtin_amdgcn_raw_buffer_load_b96(rK, voffKV, (idx - S_K) * 768, 2);
    } else {
      return __builtin_amdgcn_raw_buffer_load_b96(rV, voffKV, (idx - S_V) * 768, 2);
    }
  };

  for (int i = t; i < DM; i += NT) xs[i] = __bfloat162float(P.x[b * DM + i]);
  u32x3 ring[WIN];
#pragma unroll
  for (int s = 0; s < WIN; ++s) ring[s] = LOAD(s);

  __syncthreads();

  float aF[2][6];
#pragma unroll
  for (int m = 0; m < 2; ++m)
#pragma unroll
    for (int k = 0; k < 6; ++k) aF[m][k] = 0.f;
#pragma unroll
  for (int s = 0; s < NF; ++s) {
    const u32x3 v = ring[s % WIN];
    const float xv = xs[80 * w + 8 * (s % 10) + (lane >> 3)];
    float* a = aF[s / 10];
    a[0] += xv * blo(v.x); a[1] += xv * bhi(v.x); a[2] += xv * blo(v.y); a[3] += xv * bhi(v.y); a[4] += xv * blo(v.z); a[5] += xv * bhi(v.z);
    PIN6(a);
    ring[s % WIN] = LOAD(s + WIN);
    __builtin_amdgcn_sched_barrier(0);
  }
  float aq[6] = {0.f, 0.f, 0.f, 0.f, 0.f, 0.f};
#pragma unroll
  for (int s = S_Q; s < S_K; ++s) {
    const u32x3 v = ring[s % WIN];
    const float xv = xs[80 * w + 2 * (s - S_Q) + hf2];
    aq[0] += xv * blo(v.x); aq[1] += xv * bhi(v.x); aq[2] += xv * blo(v.y); aq[3] += xv * bhi(v.y); aq[4] += xv * blo(v.z); aq[5] += xv * bhi(v.z);
    PIN6(aq);
    ring[s % WIN] = LOAD(s + WIN);
    __builtin_amdgcn_sched_barrier(0);
  }
#pragma unroll
  for (int m = 0; m < 2; ++m)
#pragma unroll
    for (int k = 0; k < 6; ++k) {
      float vv = aF[m][k];
      vv += __shfl_xor(vv, 8); vv += __shfl_xor(vv, 16); vv += __shfl_xor(vv, 32);
      if (lane < 8) redF[(w * 2 + m) * 48 + lane * 6 + k] = vv;
    }
#pragma unroll
  for (int k = 0; k < 6; ++k) red[(w * 2 + hf2) * 192 + 6 * c + k] = aq[k];
  __syncthreads();
  if (t < 192) {
    float s = 0.f;
#pragma unroll
    for (int i = 0; i < 16; ++i) s += red[i * 192 + t];
    of[t] = bfr(s);
  } else if (t < 240) {
    const int cc = t - 192;
    float g = 0.f, u = 0.f;
#pragma unroll
    for (int i = 0; i < 8; ++i) { g += redF[(i * 2) * 48 + cc]; u += redF[(i * 2 + 1) * 48 + cc]; }
    g = bfr(g); u = bfr(u);
    const float inner = 0.7978845608028654f * (g + 0.044715f * g * g * g);
    const float ge = bfr(0.5f * g * (1.f + tanhf(inner)));
    hf[cc] = bfr(ge * u);
  }
  __syncthreads();
  unsigned q0, q1, q2;
  {
    const float* qq = of + 6 * c;
    auto pk = [](float lo, float hi) { return (__builtin_bit_cast(unsigned, lo) >> 16) | (__builtin_bit_cast(unsigned, hi) & 0xffff0000u); };
    q0 = pk(qq[0], qq[1]); q1 = pk(qq[2], qq[3]); q2 = pk(qq[4], qq[5]);
  }

  ST(1);
  constexpr float SCALE = 0.07216878364870322f;
  constexpr int NIT = NK / WIN;
#pragma unroll 1
  for (int it = 0; it < NIT; ++it) {
    const bool last = (it == NIT - 1);
#pragma unroll
    for (int j = 0; j < WIN; ++j) {
      const int slot = (S_K + j) % WIN;
      const u32x3 v = ring[slot];
      float d = dot2(v.x, q0, 0.f); d = dot2(v.y, q1, d); d = dot2(v.z, q2, d);
      d = sum32(d);
      d = bfr(bfr(d) * SCALE);
      if (c == 16) sc[128 * w + 2 * (it * WIN + j) + hf2] = d;
      ring[slot] = __builtin_amdgcn_raw_buffer_load_b96(last ? rV : rK, voffKV, last ? j * 768 : ((it + 1) * WIN + j) * 768, 2);
      __builtin_amdgcn_sched_barrier(0);
    }
  }
  __syncthreads();
  ST(2);
  {
    float sv[16];
#pragma unroll
    for (int i = 0; i < 4; ++i) {
      const float4 f = *reinterpret_cast<const float4*>(sc + lane * 16 + i * 4);
      sv[4 * i] = f.x; sv[4 * i + 1] = f.y; sv[4 * i + 2] = f.z; sv[4 * i + 3] = f.w;
    }
    float m = sv[0];
#pragma unroll
    for (int i = 1; i < 16; ++i) m = fmaxf(m, sv[i]);
    m = max64(m);
    float l = 0.f;
#pragma unroll
    for (int i = 0; i < 16; ++i) l += __expf(sv[i] - m);
    l = sum64(l);
    const float inv = 1.f / l;
    pb[128 * w + lane] = bfr(__expf(sc[128 * w + lane] - m) * inv);
    pb[128 * w + 64 + lane] = bfr(__expf(sc[128 * w + 64 + lane] - m) * inv);
  }
  float acc[6] = {0.f, 0.f, 0.f, 0.f, 0.f, 0.f};
#pragma unroll 1
  for (int it = 0; it < NIT - 1; ++it) {
#pragma unroll
    for (int j = 0; j < WIN; ++j) {
      const int slot = (S_V + j) % WIN;
      const u32x3 v = ring[slot];
      const float p = pb[128 * w + 2 * (it * WIN + j) + hf2];
      acc[0] += p * blo(v.x); acc[1] += p * bhi(v.x); acc[2] += p * blo(v.y); acc[3] += p * bhi(v.y); acc[4] += p * blo(v.z); acc[5] += p * bhi(v.z);
      PIN6(acc);
      ring[slot] = __builtin_amdgcn_raw_buffer_load_b96(rV, voffKV, ((it + 1) * WIN + j) * 768, 2);
      __builtin_amdgcn_sched_barrier(0);
    }
  }
#pragma unroll
  for (int j = 0; j < WIN; ++j) {
    const int slot = (S_V + j) % WIN;
    const u32x3 v = ring[slot];
    const float p = pb[128 * w + 2 * ((NIT - 1) * WIN + j) + hf2];
    acc[0] += p * blo(v.x); acc[1] += p * bhi(v.x); acc[2] += p * blo(v.y); acc[3] += p * bhi(v.y); acc[4] += p * blo(v.z); acc[5] += p * bhi(v.z);
  }
#pragma unroll
  for (int k = 0; k < 6; ++k) red[(w * 2 + hf2) * 192 + 6 * c + k] = acc[k];
  __syncthreads();
  if (t < 192) {
    float s = 0.f;
#pragma unroll
    for (int i = 0; i < 16; ++i) s += red[i * 192 + t];
    of[t] = bfr(s);
  }
  __syncthreads();
  ST(3);
  if (t < 480) {
    const int cc = t % 80, rg = t / 80;
    const auto rO = __builtin_amdgcn_make_buffer_rsrc((void*)P.wo, 0, NH * DH * DM * 2, 0x00020000);
    const auto rD = __builtin_amdgcn_make_buffer_rsrc((void*)P.wd, 0, FF * DM * 2, 0x00020000);
    float a8[8], b8[8];
#pragma unroll
    for (int k = 0; k < 8; ++k) { a8[k] = 0.f; b8[k] = 0.f; }
    const int vo = ((DH * h + rg) * 80 + cc) * 16;
#pragma unroll
    for (int j = 0; j < 32; ++j) {
      const u32x4 v = __builtin_amdgcn_raw_buffer_load_b128(rO, vo, j * 6 * 1280, 0);
      const float ov = of[rg + 6 * j];
      a8[0] += ov * blo(v.x); a8[1] += ov * bhi(v.x); a8[2] += ov * blo(v.y); a8[3] += ov * bhi(v.y);
      a8[4] += ov * blo(v.z); a8[5] += ov * bhi(v.z); a8[6] += ov * blo(v.w); a8[7] += ov * bhi(v.w);
    }
    const int vd = ((48 * h + rg) * 80 + cc) * 16;
#pragma unroll
    for (int j = 0; j < 8; ++j) {
      const u32x4 v = __builtin_amdgcn_raw_buffer_load_b128(rD, vd, j * 6 * 1280, 0);
      const float hv = hf[rg + 6 * j];
      b8[0] += hv * blo(v.x); b8[1] += hv * bhi(v.x); b8[2] += hv * blo(v.y); b8[3] += hv * bhi(v.y);
      b8[4] += hv * blo(v.z); b8[5] += hv * bhi(v.z); b8[6] += hv * blo(v.w); b8[7] += hv * bhi(v.w);
    }
    float4* d0 = reinterpret_cast<float4*>(red2 + rg * 640 + cc * 8);
    d0[0] = make_float4(a8[0], a8[1], a8[2], a8[3]); d0[1] = make_float4(a8[4], a8[5], a8[6], a8[7]);
    float4* d1 = reinterpret_cast<float4*>(red2 + (6 + rg) * 640 + cc * 8);
    d1[0] = make_float4(b8[0], b8[1], b8[2], b8[3]); d1[1] = make_float4(b8[4], b8[5], b8[6], b8[7]);
  }
  __syncthreads();
  for (int i = t; i < 1280; i += NT) {
    const int m = i / 640, cc = i % 640;
    float s = 0.f;
#pragma unroll
    for (int r = 0; r < 6; ++r) s += red2[(m * 6 + r) * 640 + cc];
    P.ws[((size_t)m * NB * NH + b * NH + h) * 640 + cc] = s;
  }
  ST(4);
}

__global__ void __launch_bounds__(128) k2_mix(Params P) {
  __shared__ float gs[2];
  const int b = blockIdx.x / 5, cc = (blockIdx.x % 5) * 128 + threadIdx.x;
  float s = 0.f;
  for (int i = threadIdx.x; i < DM; i += 128) s += bfr(__bfloat162float(P.x[b * DM + i]) * __bfloat162float(P.gw[i]));
  s = sum64(s);
  if ((threadIdx.x & 63) == 0) gs[threadIdx.x >> 6] = s;
  __syncthreads();
  const float z = bfr(gs[0] + gs[1]);
  const float g = bfr(1.f / (1.f + __expf(-z)));
  float a = 0.f, bb = 0.f;
#pragma unroll 8
  for (int hh = 0; hh < NH; ++hh) {
    a += P.ws[((size_t)b * NH + hh) * 640 + cc];
    bb += P.ws[((size_t)NB * NH + b * NH + hh) * 640 + cc];
  }
  a = bfr(a); bb = bfr(bb);
  const float r = bfr(g * a) + bfr(bfr(1.f - g) * bb);
  P.out[b * DM + cc] = __float2bfloat16(r);
}
}

extern "C" void kernel_launch(void* const* d_in, const int* in_sizes, int n_in, void* d_out, int out_size, void* d_ws, size_t ws_size,
                              hipStream_t stream) {
  Params P;
  P.x = (const __hip_bfloat16*)d_in[0];
  P.k = (const __hip_bfloat16*)d_in[1];
  P.v = (const __hip_bfloat16*)d_in[2];
  P.wq = (const __hip_bfloat16*)d_in[3];
  P.wo = (const __hip_bfloat16*)d_in[4];
  P.wg = (const __hip_bfloat16*)d_in[6];
  P.wu = (const __hip_bfloat16*)d_in[7];
  P.wd = (const __hip_bfloat16*)d_in[8];
  P.gw = (const __hip_bfloat16*)d_in[9];
  P.ws = (float*)d_ws;
  P.out = (__hip_bfloat16*)d_out;
  k1_attn<<<256, NT, 0, stream>>>(P);
  k2_mix<<<40, 128, 0, stream>>>(P);
}
```
